```python
import torch

def _softmax(x, axis=-1):
    return torch.softmax(x, dim=axis)
def _gelu(x):
    return torch.nn.functional.gelu(x, approximate="tanh")
def _silu(x):
    return torch.nn.functional.silu(x)
def _sigmoid(x):
    return torch.sigmoid(x)
def _layernorm(x, axis=-1, eps=1e-5):
    m = x.mean(dim=axis, keepdim=True)
    v = x.var(dim=axis, keepdim=True, unbiased=False)
    return (x - m) / torch.sqrt(v + eps)
def _scatter_add(tgt, idx, src, axis=0):
    return tgt.clone().index_add_(axis, idx, src)
def _segment_sum(src, seg, n):
    out = torch.zeros((n,) + src.shape[1:], dtype=src.dtype)
    return out.index_add_(0, seg, src)
def _pad(x, pw):
    flat = tuple(v for pair in reversed(pw) for v in pair)
    return torch.nn.functional.pad(x, flat)

kernel_name = 'standalone__linear_kernel_rfa__decode'

def setup_inputs():

    _w = (lambda a: (lambda t: t.to(torch.bfloat16) if t.is_floating_point() else t)(torch.as_tensor(a)))
    B, H, Sq, Sk, D, M = 2, 16, 1, 16384, 256, 16
    return {
        "q": _w((lambda *s: torch.randn(*s, dtype=torch.bfloat16) * 0.4)(B, H, Sq, D)),
        "k": _w((lambda *s: torch.randn(*s, dtype=torch.bfloat16) * 1.6)(B, H, Sk, D)),
        "v": _w((lambda *s: torch.randn(*s, dtype=torch.bfloat16) * 1.6)(B, H, Sk, D)),
        "omega": _w((lambda *s: torch.randn(*s, dtype=torch.bfloat16) * 1.6)(M, D)),
    }

def reference(q, k, v, omega):
    d = q.shape[-1]
    m = omega.shape[0]
    scale = float(d) ** -0.25
    qs = q * scale
    ks = k * scale
    q_proj = torch.einsum("bhsd,md->bhsm", qs, omega)
    k_proj = torch.einsum("bhsd,md->bhsm", ks, omega)
    q_sq = 0.5 * torch.einsum("bhsd,bhsd->bhs", qs, qs)[..., None]
    k_sq = 0.5 * torch.einsum("bhsd,bhsd->bhs", ks, ks)[..., None]
    inv_sqrt_m = float(m) ** -0.5
    phi_q = torch.exp(q_proj - q_sq) * inv_sqrt_m + 1e-6
    phi_k = torch.exp(k_proj - k_sq) * inv_sqrt_m + 1e-6
    kv = torch.einsum("bhsm,bhsd->bhmd", phi_k, v)
    z  = torch.einsum("bhsm->bhm", phi_k)
    num = torch.einsum("bhqm,bhmd->bhqd", phi_q, kv)
    den = torch.einsum("bhqm,bhm->bhq", phi_q, z)[..., None] + 1e-6
    return num / den
```

```cpp
#include <hip/hip_runtime.h>
#include <hip/hip_bf16.h>
#include <stdint.h>

#define ROUND_BF16 0
#define NSPLIT 8
#define NWAVE 8

namespace {

constexpr int BH = 32, SK = 16384, D = 256, M = 16;
constexpr int KEYS_PER_WG = SK / NSPLIT;
constexpr int TILES_PER_WG = KEYS_PER_WG / 16;
constexpr int TPW = TILES_PER_WG / NWAVE;
static_assert(TPW % 2 == 0 && TPW >= 2, "even tile count per wave");
constexpr int PSTRIDE = 272;

typedef unsigned u32x4 __attribute__((ext_vector_type(4)));
typedef float f32x4 __attribute__((ext_vector_type(4)));
typedef __bf16 bf16x8 __attribute__((ext_vector_type(8)));
typedef __bf16 bf16x2 __attribute__((ext_vector_type(2)));
#define GAS __attribute__((address_space(1)))

__device__ __forceinline__ float rbf(float x) {
  return x;
}
__device__ __forceinline__ float dot2(unsigned a, unsigned b, float acc) {
  return __builtin_amdgcn_fdot2_f32_bf16(__builtin_bit_cast(bf16x2, a), __builtin_bit_cast(bf16x2, b), acc, false);
}
__device__ __forceinline__ float xrow_sum(float x) {
  auto s = __builtin_amdgcn_permlane16_swap(__float_as_uint(x), __float_as_uint(x), false, false);
  x = __uint_as_float(s[0]) + __uint_as_float(s[1]);
  auto t = __builtin_amdgcn_permlane32_swap(__float_as_uint(x), __float_as_uint(x), false, false);
  return __uint_as_float(t[0]) + __uint_as_float(t[1]);
}
__device__ __forceinline__ u32x4 ldnt(const GAS u32x4* p) { return __builtin_nontemporal_load(p); }

__device__ __forceinline__ f32x4 phi4(f32x4 proj, float sq) {
  const float hs = 0.5f * rbf(sq * 0.0625f);
  f32x4 r;
#pragma unroll
  for (int j = 0; j < 4; ++j) {
    float a = rbf(rbf(proj[j] * 0.25f) - hs);
    float e = rbf(__expf(a));
    r[j] = rbf(e * 0.25f + 1e-6f);
  }
  return r;
}

__device__ __forceinline__ f32x4 feat(const u32x4 (&x)[8], const u32x4 (&om)[8]) {
  f32x4 p = {0.f, 0.f, 0.f, 0.f};
  float s0 = 0.f, s1 = 0.f;
#pragma unroll
  for (int i = 0; i < 8; ++i) {
    p = __builtin_amdgcn_mfma_f32_16x16x32_bf16(__builtin_bit_cast(bf16x8, om[i]), __builtin_bit_cast(bf16x8, x[i]), p, 0, 0, 0);
    s0 = dot2(x[i][0], x[i][0], s0);
    s1 = dot2(x[i][1], x[i][1], s1);
    s0 = dot2(x[i][2], x[i][2], s0);
    s1 = dot2(x[i][3], x[i][3], s1);
  }
  return phi4(p, xrow_sum(s0 + s1));
}

template <int N>
__device__ __forceinline__ float bcast(float w) {
  return __builtin_bit_cast(float, __builtin_amdgcn_update_dpp(0, __builtin_bit_cast(int, w), 0x150 + N, 0xf, 0xf, true));
}

template <int I>
__device__ __forceinline__ void pv_step(float w2, const u32x4& v, float (&acc)[8]) {
  const float w = bcast<2 * I>(w2);
#pragma unroll
  for (int j = 0; j < 4; ++j) {
    acc[2 * j] = fmaf(w, __builtin_bit_cast(float, v[j] << 16), acc[2 * j]);
    acc[2 * j + 1] = fmaf(w, __builtin_bit_cast(float, v[j] & 0xffff0000u), acc[2 * j + 1]);
  }
}

__global__ void __launch_bounds__(NWAVE * 64) rfa_split(const uint16_t* __restrict__ q, const uint16_t* __restrict__ k,
                                                        const uint16_t* __restrict__ v, const uint16_t* __restrict__ omega,
                                                        float* __restrict__ part) {
  __shared__ float red[NWAVE][PSTRIDE];
  const int tid = threadIdx.x, lane = tid & 63;
  const int wave = __builtin_amdgcn_readfirstlane(tid >> 6);
  const int bid = blockIdx.x;
  const int head = bid / NSPLIT, split = bid % NSPLIT;
  const int r = lane & 15, g = lane >> 4;

  u32x4 om[8], qf[8];
  {
    const GAS u32x4* op = (const GAS u32x4*)(omega) + r * 32 + g;
    const GAS u32x4* qp = (const GAS u32x4*)(q) + head * 32 + g;
#pragma unroll
    for (int i = 0; i < 8; ++i) om[i] = op[4 * i];
#pragma unroll
    for (int i = 0; i < 8; ++i) qf[i] = qp[4 * i];
  }
  const size_t row0 = (size_t)head * SK + (size_t)split * KEYS_PER_WG + wave * 16;
  const GAS u32x4* kp = (const GAS u32x4*)(k) + row0 * 32 + r * 32 + g;
  const GAS u32x4* vp = (const GAS u32x4*)(v) + row0 * 32 + lane;
  constexpr int TSTRIDE = NWAVE * 16 * 32;

  u32x4 ka[8], va[8], kb[8], vb[8];
#pragma unroll
  for (int i = 0; i < 8; ++i) ka[i] = ldnt(kp + 4 * i);
#pragma unroll
  for (int i = 0; i < 8; ++i) va[i] = ldnt(vp + 64 * i);
#pragma unroll
  for (int i = 0; i < 8; ++i) kb[i] = ldnt(kp + TSTRIDE + 4 * i);
#pragma unroll
  for (int i = 0; i < 8; ++i) vb[i] = ldnt(vp + TSTRIDE + 64 * i);
  __builtin_amdgcn_sched_barrier(0);

  const f32x4 pq = feat(qf, om);
  float acc[8];
#pragma unroll
  for (int j = 0; j < 8; ++j) acc[j] = 0.f;
  float den = 0.f;

#define TILE(KR, VR, REFILL)                                                                   \
  {                                                                                            \
    const f32x4 pk = feat(KR, om);                                                             \
    __builtin_amdgcn_sched_barrier(0);                                                         \
    if (REFILL) {                                                                              \
      _Pragma("unroll") for (int i = 0; i < 8; ++i) KR[i] = ldnt(kp + 2 * TSTRIDE + 4 * i);    \
    }                                                                                          \
    __builtin_amdgcn_sched_barrier(0);                                                         \
    float w = pq[0] * pk[0];                                                                   \
    w = fmaf(pq[1], pk[1], w);                                                                 \
    w = fmaf(pq[2], pk[2], w);                                                                 \
    w = fmaf(pq[3], pk[3], w);                                                                 \
    den += w;                                                                                  \
    w = xrow_sum(w);                                                                           \
                             \
    const float w2 = __builtin_bit_cast(float, __builtin_amdgcn_update_dpp(__builtin_bit_cast(int, w), __builtin_bit_cast(int, w), 0xB1, 0xc, 0xf, false)); \
    pv_step<0>(w2, VR[0], acc);                                                                \
    pv_step<1>(w2, VR[1], acc);                                                                \
    pv_step<2>(w2, VR[2], acc);                                                                \
    pv_step<3>(w2, VR[3], acc);                                                                \
    pv_step<4>(w2, VR[4], acc);                                                                \
    pv_step<5>(w2, VR[5], acc);                                                                \
    pv_step<6>(w2, VR[6], acc);                                                                \
    pv_step<7>(w2, VR[7], acc);                                                                \
    __builtin_amdgcn_sched_barrier(0);                                                         \
    if (REFILL) {                                                                              \
      _Pragma("unroll") for (int i = 0; i < 8; ++i) VR[i] = ldnt(vp + 2 * TSTRIDE + 64 * i);   \
    }                                                                                          \
    __builtin_amdgcn_sched_barrier(0);                                                         \
    kp += TSTRIDE;                                                                             \
    vp += TSTRIDE;                                                                             \
  }

#pragma unroll 1
  for (int t = 0; t < TPW - 2; t += 2) {
    TILE(ka, va, true)
    TILE(kb, vb, true)
  }
  TILE(ka, va, false)
  TILE(kb, vb, false)
#undef TILE

#pragma unroll
  for (int j = 0; j < 8; ++j) {
    auto t = __builtin_amdgcn_permlane32_swap(__float_as_uint(acc[j]), __float_as_uint(acc[j]), false, false);
    acc[j] = __uint_as_float(t[0]) + __uint_as_float(t[1]);
  }
  den = xrow_sum(den);
  den += __shfl_xor(den, 1);
  den += __shfl_xor(den, 2);
  den += __shfl_xor(den, 4);
  den += __shfl_xor(den, 8);
  if (lane < 32) {
    *(f32x4*)&red[wave][8 * lane] = f32x4{acc[0], acc[1], acc[2], acc[3]};
    *(f32x4*)&red[wave][8 * lane + 4] = f32x4{acc[4], acc[5], acc[6], acc[7]};
  }
  if (lane == 0) red[wave][256] = den;
  __syncthreads();
  if (tid < 257) {
    float s = 0.f;
#pragma unroll
    for (int w = 0; w < NWAVE; ++w) s += red[w][tid];
    part[(size_t)bid * PSTRIDE + tid] = s;
  }
}

__global__ void __launch_bounds__(256) rfa_combine(const float* __restrict__ part, __hip_bfloat16* __restrict__ out) {
  const int head = blockIdx.x, d = threadIdx.x;
  float num = 0.f, den = 0.f;
#pragma unroll
  for (int s = 0; s < NSPLIT; ++s) {
    num += part[(size_t)(head * NSPLIT + s) * PSTRIDE + d];
    den += part[(size_t)(head * NSPLIT + s) * PSTRIDE + 256];
  }
  const float o = rbf(num) / rbf(rbf(den) + 1e-6f);
  out[head * D + d] = __float2bfloat16(o);
}

}

extern "C" void kernel_launch(void* const* d_in, const int* in_sizes, int n_in, void* d_out, int out_size, void* d_ws,
                              size_t ws_size, hipStream_t stream) {
  const uint16_t* q = (const uint16_t*)d_in[0];
  const uint16_t* k = (const uint16_t*)d_in[1];
  const uint16_t* v = (const uint16_t*)d_in[2];
  const uint16_t* om = (const uint16_t*)d_in[3];
  float* part = (float*)d_ws;
  rfa_split<<<BH * NSPLIT, NWAVE * 64, 0, stream>>>(q, k, v, om, part);
  rfa_combine<<<BH, 256, 0, stream>>>(part, (__hip_bfloat16*)d_out);
}
```

```cpp
#include <hip/hip_runtime.h>
#include <hip/hip_bf16.h>
#include <stdint.h>

namespace {

constexpr int BH = 32, SK = 16384, D = 256;
constexpr int NSPLIT = 8, NWAVE = 4;
constexpr int T = SK / NSPLIT / NWAVE / 16;
constexpr unsigned BYTES = BH * SK * D * 2u;

constexpr int WL = 5, WM = 6, WH = 7;
constexpr int TAU = 3432;
constexpr int STEP_K = 70, STEP_V = 50;
constexpr int SLIP = 200;
constexpr int FIRST_NOM = 65, FIRST_SLACK = 10;
constexpr int T_SYNC = 14, T_APPLY = 24;
constexpr unsigned CODE_PF = 9216;

constexpr unsigned AREA = NSPLIT * 1024, PROBE0 = BH * 2 * AREA, CTL0 = 2 * PROBE0, REC = 256, R_REQ = 0, R_ACK = 128;
constexpr unsigned TIME0 = CTL0 + BH * NSPLIT * REC;
constexpr unsigned SPIN_CAP = 1u << 18;

constexpr int WAVE_LDS = 8192 + (T + 4) * 64;
constexpr int LDS_OM = NWAVE * WAVE_LDS, LDS_Q = LDS_OM + 8192, LDS_PQ = LDS_Q + 512, LDS_DEN = LDS_PQ + 32;
constexpr int LDS_TM = LDS_DEN + 16, LDS_SYNC = LDS_TM + 16, LDS_FIN = LDS_SYNC + 16, LDS_BYTES = LDS_FIN + 1024 + 16;

typedef unsigned u32x4 __attribute__((ext_vector_type(4)));
typedef unsigned u32x2 __attribute__((ext_vector_type(2)));
typedef float f32x4 __attribute__((ext_vector_type(4)));
typedef float f32x2 __attribute__((ext_vector_type(2)));
typedef short s16x4 __attribute__((ext_vector_type(4)));
typedef __bf16 bf16x8 __attribute__((ext_vector_type(8)));
typedef __bf16 bf16x2 __attribute__((ext_vector_type(2)));
#define GAS __attribute__((address_space(1)))
#define LAS __attribute__((address_space(3)))
#define SB() __builtin_amdgcn_sched_barrier(0)
#define RLX_AGENT __ATOMIC_RELAXED, __HIP_MEMORY_SCOPE_AGENT
#define NOW() ((unsigned)__builtin_amdgcn_s_memrealtime())
#define RSRC(p, n) __builtin_amdgcn_make_buffer_rsrc((void*)(p), 0, (n), 0x00020000)
constexpr int AUX_NT = 2, AUX_SC1 = 16;

constexpr unsigned B(int n) { return 1u << n; }
constexpr unsigned M_PIECE[3] = {B(15), B(17), B(18)};
constexpr unsigned M_WAVE[2] = {B(13), B(12)};
constexpr unsigned M_SPLIT[3] = {B(14), B(11), B(10)};
constexpr unsigned M_TILE[5] = {B(16), B(19) | B(14), B(20) | B(12), B(21) | B(13), B(22)};
constexpr unsigned M_HEAD[5] = {0x2u << 17, 0x4u << 17, 0xau << 17, 0x6u << 17, 0x4u << 17};
constexpr unsigned PIECE_BITS = B(15) | B(17) | B(18);
template <int N>
__device__ __forceinline__ unsigned spread(unsigned x, const unsigned (&mask)[N]) {
  unsigned r = 0;
#pragma unroll
  for (int b = 0; b < N; ++b) r ^= ((x >> b) & 1u) * mask[b];
  return r;
}

__device__ __forceinline__ unsigned pkbf(float lo, float hi) {
  const f32x2 v = {lo, hi};
  return __builtin_bit_cast(unsigned, __builtin_convertvector(v, bf16x2));
}
__device__ __forceinline__ float rbf(float x) { return __builtin_bit_cast(float, pkbf(0.f, x)); }
__device__ __forceinline__ float sq2(unsigned a, float acc) {
  return __builtin_amdgcn_fdot2_f32_bf16(__builtin_bit_cast(bf16x2, a), __builtin_bit_cast(bf16x2, a), acc, false);
}
template <int CTRL>
__device__ __forceinline__ float dpp(float x) {
  return __builtin_bit_cast(float, __builtin_amdgcn_update_dpp(0, __builtin_bit_cast(int, x), CTRL, 0xf, 0xf, true));
}
constexpr int XOR1 = 0xB1, XOR2 = 0x4E, XOR7 = 0x141, XOR8 = 0x128, BCAST = 0x150;
__device__ __forceinline__ f32x4 mfma(const u32x4& a, const u32x4& b, f32x4 c) {
  return __builtin_amdgcn_mfma_f32_16x16x32_bf16(__builtin_bit_cast(bf16x8, a), __builtin_bit_cast(bf16x8, b), c, 0, 0, 0);
}
__device__ __forceinline__ f32x4 neg_half_sq(float s) {
  return __builtin_amdgcn_mfma_f32_16x16x4f32(-0.03125f, s, f32x4{0.f, 0.f, 0.f, 0.f}, 0, 0, 0);
}
__device__ __forceinline__ float phi_arg(float proj, float nh) { return rbf(fmaf(0.25f, rbf(proj), nh)) * 1.44269504088896f; }
__device__ __forceinline__ float phi_val(float a) { return fmaf(rbf(__builtin_amdgcn_exp2f(a)), 0.25f, 1e-6f); }

template <int I>
__device__ __forceinline__ void pv_step(float w16, const u32x4& v, float (&acc)[8]) {
  const float w = dpp<BCAST + 2 * I>(w16);
#pragma unroll
  for (int j = 0; j < 4; ++j) {
    acc[2 * j] = fmaf(w, __builtin_bit_cast(float, v[j] << 16), acc[2 * j]);
    acc[2 * j + 1] = fmaf(w, __builtin_bit_cast(float, v[j] & 0xffff0000u), acc[2 * j + 1]);
  }
#pragma unroll
  for (int j = 0; j < 8; ++j) asm volatile("" : "+v"(acc[j]));
}
__device__ __forceinline__ unsigned cycles() {
  unsigned long long t;
  asm volatile("s_memtime %0\n\ts_waitcnt lgkmcnt(0)" : "=s"(t) :: "memory");
  return (unsigned)t;
}
__device__ __forceinline__ void touch(const void* p) {
  unsigned dummy;
  asm volatile("s_load_dword %0, %1, 0x0\n\ts_waitcnt lgkmcnt(0)" : "=s"(dummy) : "s"(p) : "memory");
}

__device__ __forceinline__ unsigned wait4(const LAS unsigned char* p, int lane) {
  unsigned x;
#pragma unroll 1
  for (;;) {
    x = ((const volatile LAS unsigned*)p)[lane & 3];
    if (__all(x != 0u)) return x;
    __builtin_amdgcn_s_sleep(16);
  }
}

__global__ void __launch_bounds__((NWAVE + 1) * 64, 2)
rfa_decode(const uint16_t* __restrict__ k, const uint16_t* __restrict__ v, const uint16_t* __restrict__ q,
           const uint16_t* __restrict__ omega, unsigned char* __restrict__ ws, uint16_t* __restrict__ out) {
  __shared__ __attribute__((aligned(16))) unsigned char lds_[LDS_BYTES];
  LAS unsigned char* lds = (LAS unsigned char*)lds_;
  int lane = threadIdx.x & 63;
  const int wave = __builtin_amdgcn_readfirstlane(threadIdx.x >> 6);
  const unsigned bid = blockIdx.x;
  const unsigned head = (bid & 7) | ((bid >> 3) & 24), split = (bid >> 3) & 7;

  unsigned pf0, pf1;
  {
    const unsigned po = min((unsigned)lane * 256u, CODE_PF - 132u);
    unsigned long long pc;
    asm volatile("s_cmp_lg_u32 %4, %5\n\ts_cbranch_scc1 1f\n\ts_getpc_b64 %2\n\tglobal_load_dword %0, %3, %2\n\t"
                 "global_load_dword %1, %3, %2 offset:128\n1:"
                 : "=&v"(pf0), "=&v"(pf1), "=&s"(pc) : "v"(po), "s"(wave), "n"(NWAVE) : "scc");
    SB();
  }

  if (wave == NWAVE) {
    asm volatile("" : "+v"(lane));
    const int r = lane & 15, g = lane >> 4, hi = lane >> 5, c = lane & 31;
    u32x4 om[8], qf[8];
    if (lane < (LDS_FIN - LDS_PQ) / 4) ((LAS unsigned*)(lds + LDS_PQ))[lane] = 0u;
    LAS unsigned char *hst[8], *hrd[8];
#pragma unroll
    for (int i = 0; i < 8; ++i) {
      const int rr = 2 * i + hi;
      hst[i] = lds + LDS_OM + rr * 512 + ((c ^ rr) * 16);
      hrd[i] = lds + LDS_OM + r * 512 + (((4 * i + g) ^ r) * 16);
      asm volatile("" : "+v"(hst[i]), "+v"(hrd[i]));
    }
    SB();
    asm volatile("s_waitcnt lgkmcnt(0)" ::"s"(q), "s"(omega) : "memory");
    const u32x4 q0 = ((const GAS u32x4*)q)[head * 32 + c];
#pragma unroll
    for (int i = 0; i < 8; ++i) om[i] = ((const GAS u32x4*)omega)[i * 64 + lane];
    SB();
    __builtin_amdgcn_s_barrier();
    SB();
    *(LAS u32x4*)(lds + LDS_Q + c * 16) = q0;
#pragma unroll
    for (int i = 0; i < 8; ++i) *(LAS u32x4*)hst[i] = om[i];
    asm volatile("" ::"v"(pf0), "v"(pf1) : "memory");
#pragma unroll
    for (int i = 0; i < 8; ++i) {
      om[i] = *(const LAS u32x4*)hrd[i];
      qf[i] = *(const LAS u32x4*)(lds + LDS_Q + (4 * i + g) * 16);
    }
    f32x4 p = {0.f, 0.f, 0.f, 0.f};
    float s0 = 0.f, s1 = 0.f;
#pragma unroll
    for (int i = 0; i < 8; ++i) {
      p = mfma(om[i], qf[i], p);
      s0 = sq2(qf[i][0], s0); s1 = sq2(qf[i][1], s1); s0 = sq2(qf[i][2], s0); s1 = sq2(qf[i][3], s1);
    }
    const float nh = rbf(neg_half_sq(s0 + s1)[0]);
#pragma unroll
    for (int j = 0; j < 4; ++j) p[j] = phi_val(phi_arg(p[j], nh));
    if (r == 0) *(LAS u32x2*)(lds + LDS_PQ + g * 8) = u32x2{pkbf(p[0], p[1]), pkbf(p[2], p[3])};
    asm volatile("s_waitcnt lgkmcnt(0)" ::: "memory");
    SB();
    touch((const unsigned char*)(split < 4 ? k : v) + ((size_t)head << 23) + ((split & 3) << 21));
    touch(ws + PROBE0 + head * (2 * AREA) + 1024);

    GAS unsigned char* hrec = (GAS unsigned char*)ws + CTL0 + head * (NSPLIT * REC);
    GAS unsigned char* harea = (GAS unsigned char*)ws + head * (2 * AREA);
    const int peer = lane & 7;
    GAS unsigned* reqw = (GAS unsigned*)(hrec + peer * REC + R_REQ);
    GAS unsigned long long* ackw = (GAS unsigned long long*)(hrec + R_ACK);
    unsigned nonce = 0, area = 0, dflip = 0, info = 0;
    u32x4 flip = {0u, 0u, 0u, 0u};
    if (split == 0) {
      GAS unsigned* timew = (GAS unsigned*)((GAS unsigned char*)ws + TIME0);
      const unsigned told = __hip_atomic_load(timew + c, RLX_AGENT);
      int votes = 0;
#pragma unroll 1
      for (int i = 0; i < 8; ++i) {
        const unsigned char* pa = ws + PROBE0 + head * (2 * AREA) + i * 128;
        const unsigned t0 = cycles();
        touch(pa);
        const unsigned t1 = cycles();
        touch(pa + AREA);
        const unsigned t2 = cycles();
        votes += min(max((int)(2 * t1 - t0 - t2), -300), 300);
      }
      area = votes > 0 ? AREA : 0u;
      const auto ar = RSRC(harea, 2 * AREA);
#pragma unroll
      for (int j = 0; j < NSPLIT - 1; ++j)
        flip |= (~__builtin_bit_cast(u32x4, __builtin_amdgcn_raw_buffer_load_b128(ar, lane * 16 + area, (j + 1) * 1024, AUX_SC1)) & 1u) << j;
      dflip = ~__hip_atomic_load((GAS unsigned*)(harea + area + peer * 128), RLX_AGENT) & 1u;
      asm volatile("s_waitcnt vmcnt(0)" : "+v"(flip), "+v"(dflip) :: "memory");
      const unsigned tm = wait4(lds + LDS_TM, lane), base = __builtin_amdgcn_readfirstlane(tm);
      float d = (float)(int)(tm - base);
      d += dpp<XOR1>(d);
      d += dpp<XOR2>(d);
      if (lane == 0) __hip_atomic_store(timew + head, base + (int)(d * 0.25f), RLX_AGENT);
      __builtin_amdgcn_s_sleep(100);
      const unsigned tnew = __hip_atomic_load(timew + c, RLX_AGENT);
      const int e = (int)(tnew - base);
      const bool ok = tnew != told && e > -300 && e < 300;
      float sum = ok ? (float)e : 0.f;
      sum += dpp<XOR1>(sum); sum += dpp<XOR2>(sum); sum += dpp<XOR7>(sum); sum += dpp<XOR8>(sum);
      sum = __builtin_amdgcn_mfma_f32_16x16x4f32(1.f, sum, f32x4{0.f, 0.f, 0.f, 0.f}, 0, 0, 0)[0];
      info = ((base + (int)(sum * __builtin_amdgcn_rcpf((float)max(__popcll(__ballot(ok)), 1)))) << 1) | (area != 0);
      if (lane < 8) __hip_atomic_store(ackw + peer, (unsigned long long)info << 32 | __hip_atomic_load(reqw, RLX_AGENT), RLX_AGENT);
    } else {
      if (lane == (int)split) {
        nonce = (unsigned)__hip_atomic_load(ackw + peer, RLX_AGENT) + 1u;
        __hip_atomic_store(reqw, nonce, RLX_AGENT);
      }
      wait4(lds + LDS_TM, lane);
      __builtin_amdgcn_s_sleep(127);
      if (lane == (int)split) {
#pragma unroll 1
        for (unsigned spins = 0; spins < SPIN_CAP; ++spins) {
          __builtin_amdgcn_s_sleep(110);
          const unsigned long long a = __hip_atomic_load(ackw + peer, RLX_AGENT);
          info = (unsigned)(a >> 32);
          if ((unsigned)a == nonce) break;
        }
      }
      asm volatile("" : "+v"(info) :: "memory");
      info = __builtin_amdgcn_readlane(info, split);
      area = (info & 1u) * AREA;
      flip = ~__builtin_bit_cast(u32x4, __builtin_amdgcn_raw_buffer_load_b128(RSRC(harea, 2 * AREA), lane * 16, area + split * 1024, AUX_SC1)) & 1u;
      if (lane == 0) dflip = ~__hip_atomic_load((GAS unsigned*)(harea + area + split * 128), RLX_AGENT) & 1u;
    }
    if (lane == 0) *(volatile LAS unsigned*)(lds + LDS_SYNC) = info | 1u;
    float den = __uint_as_float(wait4(lds + LDS_DEN, lane));
    den += dpp<XOR1>(den);
    den += dpp<XOR2>(den);
    float rden = 0.f;
    if (split == 0) {
      unsigned dn;
#pragma unroll 1
      for (unsigned spins = 0;; ++spins) {
        __builtin_amdgcn_s_sleep(127);
        dn = __hip_atomic_load((GAS unsigned*)(harea + area + peer * 128), RLX_AGENT) ^ dflip;
        if (__all(peer == 0 || !(dn & 1u))) break;
        if (spins == SPIN_CAP) { dn = 0x7fc00000u; break; }
        if (lane < 8) __hip_atomic_store(ackw + peer, (unsigned long long)info << 32 | __hip_atomic_load(reqw, RLX_AGENT), RLX_AGENT);
      }
      den = peer == 0 ? den : __uint_as_float(dn);
      den += dpp<XOR1>(den);
      den += dpp<XOR2>(den);
      den += dpp<XOR7>(den);
      rden = 1.0f / rbf(rbf(den) + 1e-6f);
      *(GAS u32x2*)((GAS uint16_t*)out + head * D + lane * 4) = u32x2{0u, 0u};
      asm volatile("s_waitcnt vmcnt(0)" ::: "memory");
    } else if (lane == 0) {
      __hip_atomic_store((GAS unsigned*)(harea + area + split * 128), (__float_as_uint(den) & ~1u) | dflip, RLX_AGENT);
    }
    *(LAS u32x4*)(lds + LDS_FIN + lane * 16) = flip | area;
    *(LAS float*)(lds + LDS_FIN + 1024) = rden;
    asm volatile("s_waitcnt lgkmcnt(0)" ::: "memory");
    __builtin_amdgcn_s_barrier();
    return;
  }

  const unsigned wo = (head << 23) ^ spread(head, M_HEAD) ^ spread(wave, M_WAVE) ^ spread(split, M_SPLIT);
  auto toff = [&](unsigned t) { return (wo & ~PIECE_BITS) ^ spread(t, M_TILE); };
  unsigned voff[8];
  voff[0] = lane * 16 | (wo & PIECE_BITS);
  asm volatile("" : "+v"(voff[0]));
#define LD(rs, so, i) __builtin_bit_cast(u32x4, __builtin_amdgcn_raw_buffer_load_b128(rs, voff[i], so, AUX_NT))

  const int r = lane & 15, g = lane >> 4, hi = lane >> 5, c = lane & 31;
  LAS unsigned char* kt = lds + wave * WAVE_LDS;
  LAS float* wst = (LAS float*)(kt + 8192) + r;
  const LAS float* wld = (const LAS float*)(kt + 8192) + (r ^ hi);
  LAS unsigned char *sta[8], *rda[8];

  int lag = (int)(bid >> 20) - (1 << 30);
  unsigned due, prev, now;
#define THROTTLE(ST)                                                                                                            \
  asm volatile("s_cmp_gt_i32 %0, 0\n\ts_cbranch_scc1 1f\n\ts_waitcnt vmcnt(%2)\n\ts_cmp_gt_i32 %0, %1\n\ts_cbranch_scc1 1f\n\t" \
               "s_waitcnt vmcnt(%3)\n1:\n\ts_waitcnt vmcnt(%4)"                                                                 \
               ::"s"(lag), "n"(-(ST) * 32), "n"(WM - 1), "n"(WL - 1), "n"(WH - 1) : "scc")
#define PACE(u, ST)                                            \
  { const int over = (int)((now - prev) << 5) - TAU;                                                                            \
    prev = now;                                                                                                                 \
    due += TAU + (over > SLIP * 32 ? over : 0);                                                                                 \
    lag = (int)((now << 5) - due - (__builtin_bitreverse32((u) + wave) >> 30) * ((ST) * 8)); }
#define NEXT(ptr, t)                                                         \
  { nptr = (ptr); nso2 = toff(t); asm volatile("" : "+s"(nptr), "+s"(nso2)); }

  u32x4 xb[8], kf[8], om[8];
#pragma unroll
  for (int i = 0; i < 8; ++i) {
    asm volatile("" : "=v"(om[i]));
    const int rr = 2 * i + hi;
    sta[i] = kt + rr * 512 + ((c ^ rr) * 16);
    rda[i] = kt + r * 512 + (((4 * i + g) ^ r) * 16);
    asm volatile("" : "+v"(sta[i]), "+v"(rda[i]));
  }
  unsigned so = toff(0);
  asm volatile("" : "+s"(so));
  SB();
  {
    const auto kr = RSRC(k, BYTES);
    xb[0] = LD(kr, so, 0); SB();
#pragma unroll
    for (int i = 1; i < 8; ++i) { voff[i] = voff[0] ^ spread(i, M_PIECE); asm volatile("" : "+v"(voff[i])); }
    SB();
#pragma unroll
    for (int j = 1; j < WL; ++j) { xb[j] = LD(kr, so, j); SB(); }
    const unsigned c0 = NOW();
    SB();
    __builtin_amdgcn_s_barrier();
    SB();
    asm volatile("s_waitcnt vmcnt(%0)" ::"n"(WL - 1));
    const unsigned c1 = NOW();
    SB();
#pragma unroll
    for (int j = WL; j < 8; ++j) { THROTTLE(STEP_K); SB(); xb[j] = LD(kr, so, j); SB(); }
    due = (c0 + max((int)(c1 - c0) - FIRST_SLACK, FIRST_NOM) + STEP_K) << 5;
    prev = c1 + 30;
  }
  const uint16_t* nptr = k;
  unsigned nso2 = toff(1);
  float den = 0.f, s0, s1;
  f32x4 p, sq, wv;
  u32x2 pq = {0u, 0u};
#define CH_READ() { _Pragma("unroll") for (int i = 0; i < 8; ++i) kf[i] = *(const LAS u32x4*)rda[i]; }
#define CH_MM(I0) { if ((I0) == 0) { p = f32x4{0.f, 0.f, 0.f, 0.f}; s0 = 0.f; s1 = 0.f; }                                       \
                    _Pragma("unroll") for (int i = (I0); i < (I0) + 2; ++i) { p = mfma(om[i], kf[i], p);                         \
                      s0 = sq2(kf[i][0], s0); s1 = sq2(kf[i][1], s1); s0 = sq2(kf[i][2], s0); s1 = sq2(kf[i][3], s1); }          \
                    if ((I0) == 6) sq = neg_half_sq(s0 + s1); else asm volatile("" : "+v"(s0), "+v"(s1)); }
#define CH_ARG()  { const float nh = rbf(sq[0]);                                                                                \
                    _Pragma("unroll") for (int j = 0; j < 4; ++j) p[j] = phi_arg(p[j], nh);                                     \
                    asm volatile("" : "+v"(p)); }
#define CH_VAL()  { now = NOW();                                                                                                \
                    _Pragma("unroll") for (int j = 0; j < 4; ++j) p[j] = phi_val(p[j]);                                         \
                    const u32x2 pk = {pkbf(p[0], p[1]), pkbf(p[2], p[3])};                                                      \
                    wv = __builtin_amdgcn_mfma_f32_16x16x16bf16_1k(__builtin_bit_cast(s16x4, pq), __builtin_bit_cast(s16x4, pk), \
                                                                   f32x4{0.f, 0.f, 0.f, 0.f}, 0, 0, 0); }
#define CH_W(slot) { den += wv[0]; wst[(slot) * 16] = wv[0]; asm volatile("" : "+v"(den)); }
#define K_STEP(J, SLICE) { *(LAS u32x4*)sta[J] = xb[J]; SB(); THROTTLE(STEP_K); SB(); xb[J] = LD(nr, nso, J); SB(); SLICE SB(); }
#define V_STEP(J, SLICE) { pv_step<J>(w16, xb[J], acc); SB(); THROTTLE(STEP_V); SB(); xb[J] = LD(nr, nso, J); SB(); SLICE SB(); }

#pragma unroll 1
  for (int t = 0; t < T; ++t) {
    const auto nr = RSRC(nptr, BYTES);
    const unsigned nso = nso2;
    CH_READ() SB();
    K_STEP(0, CH_MM(0)) K_STEP(1, CH_MM(2)) K_STEP(2, CH_MM(4)) K_STEP(3, CH_MM(6)) K_STEP(4, CH_ARG()) K_STEP(5, CH_VAL())
    K_STEP(6, CH_W(t) NEXT(t >= T - 2 ? v : k, t + 2)) K_STEP(7, )
    if (t == 0) {
      do {
        pq = *(const volatile LAS u32x2*)(lds + LDS_PQ + g * 8);
      } while (!__all(pq[0] != 0u && pq[1] != 0u));
      asm volatile("" ::: "memory");
#pragma unroll
      for (int i = 0; i < 8; ++i) om[i] = *(const LAS u32x4*)(rda[i] + (LDS_OM - wave * WAVE_LDS));
      den = 0.f;
    }
    if (t == T_SYNC) ((volatile LAS unsigned*)(lds + LDS_TM))[wave] = now | 1u;
    if (t == T_APPLY) {
      const unsigned sy = __builtin_amdgcn_readfirstlane(*(const volatile LAS unsigned*)(lds + LDS_SYNC));
      const unsigned due2 = (sy << 4) + STEP_K * 32 + (T_APPLY - 1 - T_SYNC) * TAU;
      const int shift = (int)(due2 - due);
      due = sy && shift > -150 * 32 && shift < 150 * 32 ? due2 : due;
    }
    PACE(t, STEP_K)
  }
  float acc[8];
#pragma unroll
  for (int j = 0; j < 8; ++j) acc[j] = 0.f;
  float w16 = wld[16], w16n = wld[32];
  const auto nr = RSRC(v, BYTES);
  {
    const unsigned nso = nso2;
    CH_READ() SB();
    V_STEP(0, CH_MM(0)) V_STEP(1, CH_MM(2)) V_STEP(2, CH_MM(4)) V_STEP(3, CH_MM(6)) V_STEP(4, CH_ARG()) V_STEP(5, CH_VAL())
    V_STEP(6, CH_W(T) NEXT(v, 2)) V_STEP(7, )
    due += (STEP_V - STEP_K) * 32;
    PACE(T, STEP_V)
  }
  den += dpp<XOR1>(den);
  den += dpp<XOR2>(den);
  den += dpp<XOR7>(den);
  den += dpp<XOR8>(den);
  if (lane == 0) ((volatile LAS float*)(lds + LDS_DEN))[wave] = den;
#pragma unroll 1
  for (int t = 1; t < T - 1; ++t) {
    const unsigned nso = nso2;
    w16 = w16n;
    w16n = wld[(t + 2) * 16];
    SB();
    V_STEP(0, ) V_STEP(1, ) V_STEP(2, ) V_STEP(3, NEXT(v, t + 2)) V_STEP(4, ) V_STEP(5, now = NOW();) V_STEP(6, ) V_STEP(7, )
    PACE(T + t, STEP_V)
  }
#define L_STEP(J) { pv_step<J>(w16n, xb[J], acc); SB(); }
  L_STEP(0) L_STEP(1) L_STEP(2) L_STEP(3) L_STEP(4) L_STEP(5) L_STEP(6) L_STEP(7)
  f32x4 m;
#pragma unroll
  for (int j = 0; j < 4; ++j) {
    const auto x = __builtin_amdgcn_permlane32_swap(__float_as_uint(acc[j]), __float_as_uint(acc[j + 4]), false, false);
    m[j] = __uint_as_float(x[0]) + __uint_as_float(x[1]);
  }
  *(LAS f32x4*)((LAS float*)kt + 8 * c + 4 * hi) = m;
  asm volatile("s_waitcnt lgkmcnt(0)" ::: "memory");
  __builtin_amdgcn_s_barrier();
  asm volatile("" ::: "memory");
  const int d4 = threadIdx.x * 4;
  const unsigned flip = *(const LAS unsigned*)(lds + LDS_FIN + d4);
  const float rden = *(const LAS float*)(lds + LDS_FIN + 1024);
  float s = *(const LAS float*)(lds + d4);
#pragma unroll
  for (int w = 1; w < NWAVE; ++w) s += *(const LAS float*)(lds + w * WAVE_LDS + d4);
  GAS unsigned char* mine = (GAS unsigned char*)ws + head * (2 * AREA) + (split ? split : 4u) * 1024;
  GAS unsigned* slot = (GAS unsigned*)(mine + ((flip & AREA) | (unsigned)d4));
  if (split != 0) {
    __hip_atomic_store(slot, (__float_as_uint(s) & ~1u) | (flip & 1u), RLX_AGENT);
    return;
  }
  float z;
  unsigned old;
#pragma unroll 1
  for (unsigned spins = 0; spins < 16 * SPIN_CAP; ++spins) {
    unsigned x[NSPLIT - 1];
#pragma unroll
    for (int j = 0; j < NSPLIT - 1; ++j) x[j] = __hip_atomic_load(slot + (j - 3) * 256, RLX_AGENT);
    SB();
    old = 0u;
    z = s;
#pragma unroll
    for (int j = 0; j < NSPLIT - 1; ++j) {
      const unsigned y = x[j] ^ ((flip >> j) & 1u);
      z += __uint_as_float(y);
      old |= y;
    }
    asm volatile("" : "+v"(z), "+v"(old));
    if (__all(!(old & 1u))) break;
  }
  z = (old & 1u) ? __uint_as_float(0x7fc00000u) : z;
  out[head * D + threadIdx.x] = (uint16_t)pkbf(rbf(z) * rden, 0.f);
}

}

extern "C" void kernel_launch(void* const* d_in, const int* in_sizes, int n_in, void* d_out, int out_size, void* d_ws,
                              size_t ws_size, hipStream_t stream) {
  const uint16_t* q = (const uint16_t*)d_in[0];
  const uint16_t* k = (const uint16_t*)d_in[1];
  const uint16_t* v = (const uint16_t*)d_in[2];
  const uint16_t* omega = (const uint16_t*)d_in[3];
  rfa_decode<<<BH * NSPLIT, (NWAVE + 1) * 64, 0, stream>>>(k, v, q, omega, (unsigned char*)d_ws, (uint16_t*)d_out);
}
```
